```python
import torch

def _softmax(x, axis=-1):
    return torch.softmax(x, dim=axis)
def _gelu(x):
    return torch.nn.functional.gelu(x, approximate="tanh")
def _silu(x):
    return torch.nn.functional.silu(x)
def _sigmoid(x):
    return torch.sigmoid(x)
def _layernorm(x, axis=-1, eps=1e-5):
    m = x.mean(dim=axis, keepdim=True)
    v = x.var(dim=axis, keepdim=True, unbiased=False)
    return (x - m) / torch.sqrt(v + eps)
def _scatter_add(tgt, idx, src, axis=0):
    return tgt.clone().index_add_(axis, idx, src)
def _segment_sum(src, seg, n):
    out = torch.zeros((n,) + src.shape[1:], dtype=src.dtype)
    return out.index_add_(0, seg, src)
def _pad(x, pw):
    flat = tuple(v for pair in reversed(pw) for v in pair)
    return torch.nn.functional.pad(x, flat)

kernel_name = 'standalone__softmax_full_bidir__sampling'

def setup_inputs():

    _w = (lambda a: (lambda t: t.to(torch.bfloat16) if t.is_floating_point() else t)(torch.as_tensor(a)))
    B, H, Sq, Skv, Dh = 8, 16, 16, 2048, 256
    return {
        "q": _w((lambda *s: torch.randn(*s, dtype=torch.bfloat16) * 0.8)(B, H, Sq, Dh)),
        "k": _w((lambda *s: torch.randn(*s, dtype=torch.bfloat16) * 0.8)(B, H, Skv, Dh)),
        "v": _w((lambda *s: torch.randn(*s, dtype=torch.bfloat16) * 0.05)(B, H, Skv, Dh)),
    }

def reference(q, k, v):
    scale = q.shape[-1] ** -0.5
    scores = torch.einsum('bhqd,bhkd->bhqk', q, k) * scale
    attn = _softmax(scores, axis=-1)
    return torch.einsum('bhqk,bhkd->bhqd', attn, v)
```

```cpp
#include <hip/hip_runtime.h>
#include <hip/hip_bf16.h>
#include <stdint.h>

typedef unsigned u32;
typedef unsigned long long u64;
typedef u32 u32x4 __attribute__((ext_vector_type(4)));
typedef u32 u32x2 __attribute__((ext_vector_type(2)));
typedef float f32x4 __attribute__((ext_vector_type(4)));
typedef float f32x2 __attribute__((ext_vector_type(2)));
typedef short s16x4 __attribute__((ext_vector_type(4)));
typedef __bf16 bf16x8_t __attribute__((ext_vector_type(8)));
typedef __bf16 bf16x2_t __attribute__((ext_vector_type(2)));
#define GAS __attribute__((address_space(1)))
#define LAS __attribute__((address_space(3)))

constexpr int NHEAD = 128, SQ = 16, SKV = 2048, DH = 256;
constexpr int ROWB = DH * 2;
constexpr u32 STAGE = 16384;
constexpr int NSTA = 30, NSTB = SKV / 32 - NSTA;
constexpr int R = 5;
constexpr int LAB = 2, LA0 = 3;
constexpr int PACE_T0 = 160, PACE_TAU = 53, PACE_HI = 100, PACE_LO = 50;
constexpr int FL = 4, CHK = 2, FL2 = 2, TL = FL + 1;
static_assert(NSTA >= TL + R && NSTB >= NSTA && LAB >= 2 && LA0 == 3 && LA0 < R, "schedule");
struct Sched { int vm[TL]; int nreq[TL]; int first[TL]; };
constexpr int ahead_of(int e) { return LA0 < e ? LA0 : e; }
constexpr Sched make_sched() {
  Sched s{};
  int q[128] = {}; int n = 0;
  for (int k = 0; k < LA0; ++k) for (int x = 0; x < 4; ++x) q[n++] = TL - 1 - k;
  for (int e = TL - 1; e >= 0; --e) {
    int last = -1;
    for (int i = 0; i < n; ++i) if (q[i] == e || (e == CHK && q[i] == -1) || e == 0) last = i;
    s.vm[e] = n - 1 - last;
    if (e == FL) q[n++] = -1;
    if (e == FL2) q[n++] = -3;
    if (e == CHK) for (int x = 0; x < 5; ++x) q[n++] = -2;
    const int prev = e == TL - 1 ? LA0 - 1 : ahead_of(e + 1) - 1;
    s.first[e] = prev + 1;
    s.nreq[e] = ahead_of(e) - prev;
    for (int k = 0; k < s.nreq[e]; ++k) for (int x = 0; x < 4; ++x) q[n++] = e - (prev + 1 + k);
  }
  return s;
}
constexpr Sched SCH = make_sched();
constexpr int AUX_SC1 = 16;
constexpr float C1 = 0.0625f * 1.4426950408889634f;

constexpr size_t CTRL_STRIDE = 1024, WS_SLOT = NHEAD * CTRL_STRIDE;
constexpr u32 WSLOT_BYTES = 4 * 1024 + 256;
constexpr u32 WSLOT_STRIDE = 4608;

constexpr u32 LDS_RING = 0;
constexpr u32 LDS_S = R * STAGE;
constexpr u32 LDS_Q = LDS_S + NSTB * 2048 - 8192;
constexpr u32 LDS_P = LDS_S + NSTB * 2048;
constexpr u32 LDS_ST = LDS_P + 2048;
constexpr u32 LDS_BYTES = LDS_ST + 256;
static_assert(LDS_BYTES <= 163840 && LDS_BYTES > 81920, "LDS: one workgroup per CU");

__device__ __forceinline__ f32x4 mfma32(u32x4 a, u32x4 b, f32x4 c) {
  return __builtin_amdgcn_mfma_f32_16x16x32_bf16(__builtin_bit_cast(bf16x8_t, a), __builtin_bit_cast(bf16x8_t, b), c, 0, 0, 0);
}
__device__ __forceinline__ u32 pk_bf16(float lo, float hi) {
  f32x2 v = {lo, hi};
  return __builtin_bit_cast(u32, __builtin_convertvector(v, bf16x2_t));
}
__device__ __forceinline__ float fu(u32 x) { return __builtin_bit_cast(float, x); }
__device__ __forceinline__ u32 uf(float x) { return __builtin_bit_cast(u32, x); }
__device__ __forceinline__ float xg_max(float x) {
  auto s = __builtin_amdgcn_permlane16_swap(uf(x), uf(x), false, false);
  x = fmaxf(fu(s[0]), fu(s[1]));
  auto t = __builtin_amdgcn_permlane32_swap(uf(x), uf(x), false, false);
  return fmaxf(fu(t[0]), fu(t[1]));
}

__device__ __forceinline__ void dma_page(__amdgpu_buffer_rsrc_t r, u32 soff, u32 lds, u32 v0, u32 v1, u32 v2, u32 v3) {
  asm volatile(
      "s_mov_b32 m0, %0\n\t"
      "s_nop 0\n\t"
      "buffer_load_dwordx4 %3, %1, %2 offen nt lds\n\t"
      "buffer_load_dwordx4 %4, %1, %2 offen offset:1024 nt lds\n\t"
      "buffer_load_dwordx4 %5, %1, %2 offen offset:2048 nt lds\n\t"
      "buffer_load_dwordx4 %6, %1, %2 offen offset:3072 nt lds"
      :: "s"(lds), "s"(r), "s"(soff), "v"(v0), "v"(v1), "v"(v2), "v"(v3) : "memory");
}
__device__ __forceinline__ void dma_half_page(__amdgpu_buffer_rsrc_t r, u32 lds, u32 v0, u32 v1) {
  asm volatile(
      "s_mov_b32 m0, %0\n\t"
      "s_nop 0\n\t"
      "buffer_load_dwordx4 %2, %1, 0 offen nt lds\n\t"
      "buffer_load_dwordx4 %3, %1, 0 offen offset:1024 nt lds"
      :: "s"(lds), "s"(r), "v"(v0), "v"(v1) : "memory");
}
template <int VM> __device__ __forceinline__ void wait_barrier() {
  asm volatile("s_waitcnt vmcnt(%0) lgkmcnt(0)\n\ts_barrier" :: "n"(VM) : "memory");
}

__global__ void __launch_bounds__(256, 2)
attn_kernel(const unsigned char* __restrict__ Q, const unsigned char* __restrict__ K, const unsigned char* __restrict__ V,
            unsigned char* __restrict__ out, unsigned char* __restrict__ ws, const u64* __restrict__ wsx) {
  extern __shared__ __attribute__((aligned(16))) unsigned char smem_[];
  LAS unsigned char* smem = (LAS unsigned char*)smem_;
  const int tid = threadIdx.x, lane = tid & 63, wave = __builtin_amdgcn_readfirstlane(tid >> 6);
  const int g = lane >> 4, c = lane & 15;
  const int b = blockIdx.x, head = (b & 31) | ((b >> 6) << 5), role = (b >> 5) & 1;
  const int nst = role ? NSTB : NSTA;
  const size_t kvoff = ((size_t)head * SKV + (role ? NSTA * 32 : 0)) * ROWB + (size_t)wave * 4096;
  const u32 kvbytes = (u32)nst * STAGE - (u32)wave * 4096u;
  const __amdgpu_buffer_rsrc_t rk = __builtin_amdgcn_make_buffer_rsrc((void*)(K + kvoff), 0, kvbytes, 0x00020000);
  const u32 lds0 = (u32)(size_t)smem;
  const u32 ldsw = lds0 + LDS_RING + (u32)wave * 4096u;
  const int hi = lane >> 5, ch = lane & 31;
  u32 vo[4];
#pragma unroll
  for (int x = 0; x < 4; ++x) vo[x] = hi * ROWB + ((ch ^ (2 * (2 * x + hi))) * 16);

  dma_page(rk, 0, ldsw, vo[0], vo[1], vo[2], vo[3]);
  {
    const __amdgpu_buffer_rsrc_t rq = __builtin_amdgcn_make_buffer_rsrc(
        (void*)(Q + (size_t)head * SQ * ROWB + wave * 2048), 0, 2048, 0x00020000);
    const int x0 = 2 * (wave & 1);
    dma_half_page(rq, lds0 + LDS_Q + (u32)wave * 2048u, hi * ROWB + ((ch ^ (2 * (2 * x0 + hi))) * 16),
                  hi * ROWB + ((ch ^ (2 * (2 * x0 + 2 + hi))) * 16));
  }
#pragma unroll
  for (int s = 1; s < LAB; ++s) dma_page(rk, s * STAGE, ldsw + s * STAGE, vo[0], vo[1], vo[2], vo[3]);

  __builtin_amdgcn_sched_barrier(0);
  const u64 tstart = __builtin_amdgcn_s_memrealtime();
  const __amdgpu_buffer_rsrc_t rv = __builtin_amdgcn_make_buffer_rsrc((void*)(V + kvoff), 0, kvbytes, 0x00020000);
  const u64 epoch = wsx[(size_t)head * (CTRL_STRIDE / 8)] + 1;

  u32 koff[4], voff[4];
  {
    const u32 y = g ^ (2 * (c & 7));
#pragma unroll
    for (int u = 0; u < 4; ++u) koff[u] = c * ROWB + (((4 * u) ^ y) * 16);
    const int q4 = (lane >> 2) & 3, p = lane & 3, row = 4 * g + q4, x = 2 * (row & 7);
#pragma unroll
    for (int n = 0; n < 4; ++n) voff[n] = row * ROWB + ((((8 * wave + 2 * n) ^ x) + (p >> 1)) * 16) + 8 * (p & 1);
  }
  const int tile = wave & 1, par = wave >> 1;

  u32 cs = 0;
  u32 fs = LAB * STAGE;
  auto advance = [&]() { cs = cs + STAGE == R * STAGE ? 0 : cs + STAGE; };
  auto request = [&](__amdgpu_buffer_rsrc_t r, u32 stage) {
    dma_page(r, stage * STAGE, ldsw + fs, vo[0], vo[1], vo[2], vo[3]);
    fs = fs + STAGE == R * STAGE ? 0 : fs + STAGE;
  };
  int nxt = LAB, la = LAB;
  u64 now = tstart;
  auto wait_stage = [&](int i) {
    const int younger = nxt - 1 - i;
    if (younger >= 3) wait_barrier<12>(); else if (younger == 2) wait_barrier<8>(); else wait_barrier<4>();
    asm volatile("" : "+s"(now));
    const int lag = (int)(u32)(now - tstart) - PACE_T0 - i * PACE_TAU;
    la = lag > PACE_HI ? LA0 : (lag < PACE_LO ? LAB : la);
  };
  auto refill = [&](int i) {
    while (nxt <= i + la) {
      const bool isv = nxt >= nst;
      request(isv ? rv : rk, (u32)(isv ? nxt - nst : nxt));
      ++nxt;
    }
  };
  u32x4 q[8];
  float mx = -INFINITY;

  wait_barrier<4 * (LAB - 1)>();
#pragma unroll
  for (int s = 0; s < 8; ++s) q[s] = *(const LAS u32x4*)(smem + LDS_Q + koff[s & 3] + (s >> 2) * 256);
  asm volatile("" :: "s"(epoch));
  for (int j = 0;;) {
    refill(j);
    if ((j & 1) == par) {
      LAS unsigned char* kb = smem + LDS_RING + cs + tile * 8192;
      u32x4 kf[8];
#pragma unroll
      for (int s = 0; s < 8; ++s) kf[s] = *(const LAS u32x4*)(kb + koff[s & 3] + (s >> 2) * 256);
      __builtin_amdgcn_sched_barrier(0);
      f32x4 d0 = {0.f, 0.f, 0.f, 0.f}, d1 = d0;
#pragma unroll
      for (int s = 0; s < 8; s += 2) { d0 = mfma32(kf[s], q[s], d0); d1 = mfma32(kf[s + 1], q[s + 1], d1); }
      d0 += d1;
      mx = fmaxf(fmaxf(mx, fmaxf(d0[0], d0[1])), fmaxf(d0[2], d0[3]));
      *(LAS f32x4*)(smem + LDS_S + j * 2048 + tile * 1024 + lane * 16) = d0;
    }
    advance();
    now = __builtin_amdgcn_s_memrealtime();
    if (++j == nst) break;
    wait_stage(j);
  }
  LAS float* st = (LAS float*)(smem + LDS_ST);
  mx = xg_max(mx) * C1;
  if (g == 0) st[wave * 16 + c] = mx;
  asm volatile("s_waitcnt lgkmcnt(0)\n\ts_barrier" ::: "memory");
  refill(nst);
  const float M = fmaxf(fmaxf(st[c], st[16 + c]), fmaxf(st[32 + c], st[48 + c]));
  LAS unsigned char* sbase = smem + LDS_S + tile * 1024 + lane * 16 + par * 8;
  LAS unsigned char* pbase = smem + LDS_P + lane * 16 + tile * 8 + par * 4;
  auto put_p = [&](int j, f32x2 s) {
    *(LAS u32*)(pbase + (j & 1) * 1024) =
        pk_bf16(__builtin_amdgcn_exp2f(fmaf(s[0], C1, -M)), __builtin_amdgcn_exp2f(fmaf(s[1], C1, -M)));
  };
  put_p(0, *(const LAS f32x2*)(sbase));

  f32x4 o[4], ol = {0.f, 0.f, 0.f, 0.f};
#pragma unroll
  for (int n = 0; n < 4; ++n) o[n] = f32x4{0.f, 0.f, 0.f, 0.f};
  const u32x4 ones = {0x3f803f80u, 0x3f803f80u, 0x3f803f80u, 0x3f803f80u};
  auto vstep = [&](int j, bool next, auto&& mid) {
    LAS unsigned char* vb = smem + LDS_RING + cs;
    f32x2 s = {0.f, 0.f};
    if (next) s = *(const LAS f32x2*)(sbase + (j + 1) * 2048);
    const u32x4 p = *(const LAS u32x4*)(smem + LDS_P + (j & 1) * 1024 + lane * 16);
    u32x2 b0[4], b1[4];
#pragma unroll
    for (int n = 0; n < 4; ++n) {
      b0[n] = __builtin_bit_cast(u32x2, __builtin_amdgcn_ds_read_tr16_b64_v4i16((LAS s16x4*)(vb + voff[n])));
      b1[n] = __builtin_bit_cast(u32x2, __builtin_amdgcn_ds_read_tr16_b64_v4i16((LAS s16x4*)(vb + voff[n] + 8192)));
    }
    __builtin_amdgcn_sched_barrier(0);
    if (next) put_p(j + 1, s);
    mid();
    ol = mfma32(ones, p, ol);
#pragma unroll
    for (int n = 0; n < 4; ++n) o[n] = mfma32(u32x4{b0[n][0], b0[n][1], b1[n][0], b1[n][1]}, p, o[n]);
    advance();
  };
  int j = 0;
  for (; j + TL < nst; ++j) {
    wait_stage(nst + j);
    if (j + TL + 1 == nst) la = LA0;
    refill(nst + j);
    vstep(j, true, [] {});
    now = __builtin_amdgcn_s_memrealtime();
  }
  GAS u64* ctrl = (GAS u64*)(ws + (size_t)head * CTRL_STRIDE);
  const u64 tag_mine = (epoch * 2 + (u64)role) * 0x9E3779B97F4A7C15ull;
  const u64 tag_other = (epoch * 2 + (u64)(role ^ 1)) * 0x9E3779B97F4A7C15ull;
  const size_t slot0 = WS_SLOT + ((size_t)head * 8 + wave) * WSLOT_STRIDE;
  unsigned char* ppart = ws + slot0 + (size_t)(role ^ 1) * 4 * WSLOT_STRIDE;
  const __amdgpu_buffer_rsrc_t rpart = __builtin_amdgcn_make_buffer_rsrc((void*)ppart, 0, WSLOT_BYTES, 0x00020000);
  GAS u64* wword = ctrl + 16 * (wave + 1);
  const __amdgpu_buffer_rsrc_t rout = __builtin_amdgcn_make_buffer_rsrc((void*)(out + (size_t)head * SQ * ROWB), 0, SQ * ROWB, 0x00020000);
  u32 ooff = c * ROWB + (64 * wave + 16 * (g & 1) + 8 * (g >> 1)) * 2;
  asm volatile("" : "+v"(ooff));
  u64 flagv, flagv2;
  bool got = false;
  u32x4 pp[4];
  u32x4 ps;
  float sm = 1.f, lps = 0.f;
  auto prescale = [&]() {
#pragma clang fp contract(off)
    const float mp = fmaxf(fu(ps[0]), fu(ps[2])), mt = fmaxf(M, mp);
    sm = __builtin_amdgcn_exp2f(M - mt);
    const float sp = __builtin_amdgcn_exp2f(mp - mt);
    lps = fminf(fu(ps[1]), fu(ps[3])) * sp;
#pragma unroll
    for (int n = 0; n < 4; ++n)
#pragma unroll
      for (int ii = 0; ii < 4; ++ii) pp[n][ii] = uf(fu(pp[n][ii]) * sp);
    asm volatile("" : "+v"(pp[0]), "+v"(pp[1]), "+v"(pp[2]), "+v"(pp[3]), "+v"(sm), "+v"(lps));
  };
#define TAIL_STEP(E)                                                                                    \
  {                                                                                                     \
    wait_barrier<SCH.vm[E]>();                                                                          \
    if constexpr ((E) == FL)                                                                            \
      asm volatile("global_load_dwordx2 %0, %1, %2 sc1" : "=v"(flagv) : "v"(0u), "s"(wword) : "memory"); \
    if constexpr ((E) == FL2)                                                                           \
      asm volatile("global_load_dwordx2 %0, %1, %2 sc1" : "=v"(flagv2) : "v"(0u), "s"(wword) : "memory"); \
    if constexpr ((E) == CHK) {                                                                         \
      asm volatile("" : "+v"(flagv));                                                                   \
      got = __builtin_amdgcn_readfirstlane((u32)(flagv == tag_other)) != 0u;                            \
      const __amdgpu_buffer_rsrc_t rg = __builtin_amdgcn_make_buffer_rsrc((void*)ppart, 0, got ? WSLOT_BYTES : 0u, 0x00020000); \
      asm volatile("buffer_load_dwordx4 %0, %5, %7, 0 offen sc1\n\t"                                    \
                   "buffer_load_dwordx4 %1, %5, %7, 0 offen offset:1024 sc1\n\t"                        \
                   "buffer_load_dwordx4 %2, %5, %7, 0 offen offset:2048 sc1\n\t"                        \
                   "buffer_load_dwordx4 %3, %5, %7, 0 offen offset:3072 sc1\n\t"                        \
                   "buffer_load_dwordx4 %4, %6, %7, 0 offen sc1"                                        \
                   : "=&v"(pp[0]), "=&v"(pp[1]), "=&v"(pp[2]), "=&v"(pp[3]), "=&v"(ps)                  \
                   : "v"(lane * 16), "v"(4096 + c * 16), "s"(rg) : "memory");                           \
    }                                                                                                   \
    if constexpr ((E) == 0)                                                                             \
      asm volatile("" : "+v"(pp[0]), "+v"(pp[1]), "+v"(pp[2]), "+v"(pp[3]), "+v"(ps), "+v"(flagv2));    \
    __builtin_amdgcn_sched_barrier(0);                                                                  \
    _Pragma("unroll") for (int k_ = 0; k_ < SCH.nreq[E]; ++k_) request(rv, (u32)(j + SCH.first[E] + k_)); \
    if constexpr ((E) == 0) vstep(j, false, prescale); else vstep(j, true, [] {});                      \
    ++j;                                                                                                \
  }
  static_assert(TL == 5, "written-out steps");
  TAIL_STEP(4) TAIL_STEP(3) TAIL_STEP(2) TAIL_STEP(1) TAIL_STEP(0)
#undef TAIL_STEP

  const float L = ol[0];
  if (!got && __builtin_amdgcn_readfirstlane((u32)(flagv2 == tag_other)) != 0u) {
    __builtin_amdgcn_fence(__ATOMIC_ACQUIRE, "agent");
#pragma unroll
    for (int k = 0; k < 4; ++k) pp[k] = __builtin_amdgcn_raw_buffer_load_b128(rpart, lane * 16 + k * 1024, 0, AUX_SC1);
    ps = __builtin_amdgcn_raw_buffer_load_b128(rpart, 4096 + c * 16, 0, AUX_SC1);
    prescale();
    got = true;
  }
  if (!got) {
    const __amdgpu_buffer_rsrc_t rmine = __builtin_amdgcn_make_buffer_rsrc((void*)(ws + slot0 + (size_t)role * 4 * WSLOT_STRIDE), 0, WSLOT_BYTES, 0x00020000);
#pragma unroll
    for (int n = 0; n < 4; ++n)
      __builtin_amdgcn_raw_buffer_store_b128(__builtin_bit_cast(u32x4, o[n]), rmine, lane * 16 + n * 1024, 0, AUX_SC1);
    if (g == 0) __builtin_amdgcn_raw_buffer_store_b128(u32x4{uf(M), uf(L), uf(M), uf(L)}, rmine, 4096 + c * 16, 0, AUX_SC1);
    asm volatile("s_waitcnt vmcnt(0)" ::: "memory");
    u32 second = 0;
    if (lane == 0) {
      const u64 old = __hip_atomic_exchange(wword, tag_mine, __ATOMIC_RELAXED, __HIP_MEMORY_SCOPE_AGENT);
      second = old == tag_other ? 1u : 0u;
    }
    if (__builtin_amdgcn_readfirstlane(second) == 0u) return;
    __builtin_amdgcn_fence(__ATOMIC_ACQUIRE, "agent");
#pragma unroll
    for (int k = 0; k < 4; ++k) pp[k] = __builtin_amdgcn_raw_buffer_load_b128(rpart, lane * 16 + k * 1024, 0, AUX_SC1);
    ps = __builtin_amdgcn_raw_buffer_load_b128(rpart, 4096 + c * 16, 0, AUX_SC1);
    prescale();
  }
  {
#pragma clang fp contract(off)
    const float inv = __builtin_amdgcn_rcpf(L * sm + lps);
    u32 w[4][2];
#pragma unroll
    for (int n = 0; n < 4; ++n) {
      float r[4];
#pragma unroll
      for (int ii = 0; ii < 4; ++ii) r[ii] = (o[n][ii] * sm + fu(pp[n][ii])) * inv;
      w[n][0] = pk_bf16(r[0], r[1]);
      w[n][1] = pk_bf16(r[2], r[3]);
    }
#pragma unroll
    for (int n = 0; n < 4; n += 2) {
      auto x0 = __builtin_amdgcn_permlane16_swap(w[n][0], w[n + 1][0], false, false);
      auto x1 = __builtin_amdgcn_permlane16_swap(w[n][1], w[n + 1][1], false, false);
      __builtin_amdgcn_raw_buffer_store_b128(u32x4{x0[0], x1[0], x0[1], x1[1]}, rout, ooff + n * 32, 0, 0);
    }
    if (tid == 0) *ctrl = epoch;
  }
}

extern "C" void kernel_launch(void* const* d_in, const int* in_sizes, int n_in, void* d_out, int out_size, void* d_ws,
                              size_t ws_size, hipStream_t stream) {
  (void)in_sizes; (void)n_in; (void)out_size; (void)ws_size;
  (void)hipFuncSetAttribute((const void*)attn_kernel, hipFuncAttributeMaxDynamicSharedMemorySize, LDS_BYTES);
  hipLaunchKernelGGL(attn_kernel, dim3(256), dim3(256), LDS_BYTES, stream, (const unsigned char*)d_in[0],
                     (const unsigned char*)d_in[1], (const unsigned char*)d_in[2], (unsigned char*)d_out, (unsigned char*)d_ws,
                     (const u64*)d_ws);
}
```
